```python
import torch

def _softmax(x, axis=-1):
    return torch.softmax(x, dim=axis)
def _gelu(x):
    return torch.nn.functional.gelu(x, approximate="tanh")
def _silu(x):
    return torch.nn.functional.silu(x)
def _sigmoid(x):
    return torch.sigmoid(x)
def _layernorm(x, axis=-1, eps=1e-5):
    m = x.mean(dim=axis, keepdim=True)
    v = x.var(dim=axis, keepdim=True, unbiased=False)
    return (x - m) / torch.sqrt(v + eps)
def _scatter_add(tgt, idx, src, axis=0):
    return tgt.clone().index_add_(axis, idx, src)
def _segment_sum(src, seg, n):
    out = torch.zeros((n,) + src.shape[1:], dtype=src.dtype)
    return out.index_add_(0, seg, src)
def _pad(x, pw):
    flat = tuple(v for pair in reversed(pw) for v in pair)
    return torch.nn.functional.pad(x, flat)

kernel_name = 'gated_mix__gqa_block_cached__glu_gated_geglu__decode'

def setup_inputs():

    _w = (lambda a: (lambda t: t.to(torch.bfloat16) if t.is_floating_point() else t)(torch.as_tensor(a)))
    def _A_setup():
        B, Sq, D = 2, 1, 576
        Hq, Hkv, Sk, Dh = 32, 16, 32768, 128
        assert Hq % Hkv == 0, (Hq, Hkv)
        return {
            "x": _w((lambda *s: torch.randn(*s, dtype=torch.bfloat16) * 0.05)(B, Sq, D)),
            "k_cache": _w((lambda *s: torch.randn(*s, dtype=torch.bfloat16) * 6.4)(B, Hkv, Sk, Dh)),
            "v_cache": _w((lambda *s: torch.randn(*s, dtype=torch.bfloat16) * 0.05)(B, Hkv, Sk, Dh)),
            "wq": _w((lambda *s: torch.randn(*s, dtype=torch.bfloat16) * 6.4)(D, Hq * Dh) * (D ** -0.5)),
            "wo": _w((lambda *s: torch.randn(*s, dtype=torch.bfloat16) * 0.05)(Hq * Dh, D) * ((Hq * Dh) ** -0.5)),
        }
    _A_in = _A_setup()

    def _B_setup():
        B, S, Dm, Df = 2, 1, 576, 4608
        return {
            "x": _w((lambda *s: torch.randn(*s, dtype=torch.bfloat16) * 0.05)(B, S, Dm)),
            "w_gate": _w((lambda *s: torch.randn(*s, dtype=torch.bfloat16) * 0.4)(Dm, Df) * (Dm ** -0.5)),
            "w_up": _w((lambda *s: torch.randn(*s, dtype=torch.bfloat16) * 0.4)(Dm, Df) * (Dm ** -0.5)),
            "w_down": _w((lambda *s: torch.randn(*s, dtype=torch.bfloat16) * 0.4)(Df, Dm) * (Df ** -0.5)),
        }
    _B_in = _B_setup()

    return {
        "A_x": _A_in["x"],
        "A_k_cache": _A_in["k_cache"],
        "A_v_cache": _A_in["v_cache"],
        "A_wq": _A_in["wq"],
        "A_wo": _A_in["wo"],
        "B_x": _B_in["x"],
        "B_w_gate": _B_in["w_gate"],
        "B_w_up": _B_in["w_up"],
        "B_w_down": _B_in["w_down"],
        "gate_w": _w((lambda *s: torch.randn(*s, dtype=torch.bfloat16) * 0.05)(576) * 16.0),
    }

def reference(A_x, A_k_cache, A_v_cache, A_wq, A_wo, B_x, B_w_gate, B_w_up, B_w_down, gate_w):
    def _A_ref(x, k_cache, v_cache, wq, wo):
        B, Sq, D = x.shape
        Hq, Dh = 32, 128
        Hkv = k_cache.shape[1]
        rep = Hq // Hkv
        q = (lambda x, *d: x.permute(*d))((x @ wq).reshape(B, Sq, Hq, Dh), 0, 2, 1, 3)
        k = (lambda x, n, axis: torch.repeat_interleave(x, n, dim=axis))(k_cache, rep, axis=1)
        v = (lambda x, n, axis: torch.repeat_interleave(x, n, dim=axis))(v_cache, rep, axis=1)
        scores = torch.einsum("bhqd,bhkd->bhqk", q, k) * (Dh ** -0.5)
        p = _softmax(scores, axis=-1)
        o = torch.einsum("bhqk,bhkd->bhqd", p, v)
        return (lambda x, *d: x.permute(*d))(o, 0, 2, 1, 3).reshape(B, Sq, Hq * Dh) @ wo

    def _B_ref(x, w_gate, w_up, w_down):
        g = torch.einsum("bsd,df->bsf", x, w_gate)
        u = torch.einsum("bsd,df->bsf", x, w_up)
        h = _gelu(g) * u
        return torch.einsum("bsf,fd->bsd", h, w_down)

    x = A_x
    g = _sigmoid((x * gate_w).sum(-1, keepdims=True))
    a = _A_ref(x, A_k_cache, A_v_cache, A_wq, A_wo)
    b = _B_ref(x, B_w_gate, B_w_up, B_w_down)
    return g * a + (1 - g) * b
```

```cpp
#include <hip/hip_runtime.h>
#include <hip/hip_bf16.h>
#include <cstdint>

namespace {

constexpr int BATCH = 2, DM = 576, HQ = 32, HKV = 16, SK = 32768, DH = 128, DF = 4608;
constexpr int NQ = HQ * DH;
constexpr int NSPLIT = 8, AW = 8, NTHR = AW * 64;
constexpr int KEYS_PER_WG = SK / NSPLIT;
constexpr int TILES_PER_WAVE = KEYS_PER_WG / 16 / AW;
constexpr int NATT = BATCH * HKV * NSPLIT;
constexpr int NMLP = DF / 128;
constexpr unsigned NGROUPS = BATCH * HKV;
constexpr unsigned NARRIVE = NGROUPS + NMLP;

constexpr size_t WS_ML = 0;
constexpr size_t WS_PO = WS_ML + (size_t)BATCH * HQ * NSPLIT * 2 * 4;
constexpr size_t WS_PA = WS_PO + (size_t)BATCH * HQ * NSPLIT * DH * 4;
constexpr size_t WS_PB = WS_PA + (size_t)NGROUPS * DM * 4;

__device__ unsigned g_ticket[64];

using bf16x8 = __attribute__((ext_vector_type(8))) short;
using f32x4 = __attribute__((ext_vector_type(4))) float;
using f32x2 = __attribute__((ext_vector_type(2))) float;
using u32x4 = __attribute__((ext_vector_type(4))) unsigned;

__device__ __forceinline__ float bf_lo(unsigned u) { return __uint_as_float(u << 16); }
__device__ __forceinline__ float bf_hi(unsigned u) { return __uint_as_float(u & 0xffff0000u); }
__device__ __forceinline__ float bf2f(unsigned short h) { return __uint_as_float((unsigned)h << 16); }
__device__ __forceinline__ unsigned short f2bf(float f) {
  unsigned u = __float_as_uint(f);
  u += 0x7fffu + ((u >> 16) & 1u);
  return (unsigned short)(u >> 16);
}
__device__ __forceinline__ float rbf(float f) { return bf2f(f2bf(f)); }
template <int CTRL> __device__ __forceinline__ float dpp(float x) {
  return __builtin_bit_cast(float, __builtin_amdgcn_mov_dpp(__builtin_bit_cast(int, x), CTRL, 0xf, 0xf, true));
}
constexpr int XOR1 = 0xB1;
__device__ __forceinline__ void st_agent(float* p, float v) { __hip_atomic_store(p, v, __ATOMIC_RELAXED, __HIP_MEMORY_SCOPE_AGENT); }
#define FMA8(A_, X_, W_) do { _Pragma("unroll") for (int k_ = 0; k_ < 4; ++k_) { \
    A_[2 * k_] = fmaf((X_), bf_lo((W_)[k_]), A_[2 * k_]); A_[2 * k_ + 1] = fmaf((X_), bf_hi((W_)[k_]), A_[2 * k_ + 1]); } } while (0)

constexpr int SM_FLOATS = AW * 4 * 2 * DH + 256;

__global__ void __launch_bounds__(NTHR, 4) k_fused(const unsigned short* __restrict__ x, const unsigned short* __restrict__ kc,
                                                const unsigned short* __restrict__ vc, const unsigned short* __restrict__ wq,
                                                const unsigned short* __restrict__ wo, const unsigned short* __restrict__ wg,
                                                const unsigned short* __restrict__ wu, const unsigned short* __restrict__ wd,
                                                const unsigned short* __restrict__ gw, unsigned short* __restrict__ out,
                                                unsigned char* __restrict__ ws) {
  __shared__ __attribute__((aligned(16))) float sm[SM_FLOATS];
  __shared__ float xs[2][DM];
  __shared__ __attribute__((aligned(16))) unsigned short s_q[2 * DH];
  __shared__ unsigned s_flag;
  const int tid = threadIdx.x, lane = tid & 63, wave = __builtin_amdgcn_readfirstlane(tid >> 6);
  const int id = blockIdx.x;

  if (id < NATT) {
    const int pair = (id & 7) + 8 * ((id >> 3) & 3), split = id >> 5;
    const int b = pair >> 4, kvh = pair & 15;
    const int g = lane >> 4, c = lane & 15;
    constexpr float C2 = 0.088388347648318440f * 1.4426950408889634f;
    const size_t head_off = ((size_t)pair * SK + (size_t)split * KEYS_PER_WG) * DH;
    const unsigned short* kp = kc + head_off + (size_t)c * DH + 8 * g;
    const unsigned short* vp = vc + head_off + (size_t)(4 * g) * DH + 8 * c;
    u32x4 Kn[4], Vn[4];
#define ISSUE(t_) do { const size_t o_ = (size_t)(t_) * 16 * DH; \
    _Pragma("unroll") for (int j_ = 0; j_ < 4; ++j_) Kn[j_] = __builtin_nontemporal_load(reinterpret_cast<const u32x4*>(kp + o_ + 32 * j_)); \
    _Pragma("unroll") for (int j_ = 0; j_ < 4; ++j_) Vn[j_] = __builtin_nontemporal_load(reinterpret_cast<const u32x4*>(vp + o_ + (size_t)j_ * DH)); } while (0)
    ISSUE(wave);
    {
      for (int i = tid; i < DM; i += NTHR) xs[0][i] = bf2f(x[b * DM + i]);
      const int cc = tid & 31, rg = tid >> 5;
      const unsigned short* wp = wq + (size_t)rg * NQ + kvh * 256 + cc * 8;
      float a[8];
#pragma unroll
      for (int k = 0; k < 8; ++k) a[k] = 0.f;
      __syncthreads();
#pragma unroll 1
      for (int bt = 0; bt < 3; ++bt) {
        u32x4 wv[12];
#pragma unroll
        for (int i = 0; i < 12; ++i) wv[i] = *reinterpret_cast<const u32x4*>(wp + (size_t)(16 * (12 * bt + i)) * NQ);
#pragma unroll
        for (int i = 0; i < 12; ++i) { const float xv = xs[0][rg + 16 * (12 * bt + i)]; FMA8(a, xv, wv[i]); }
      }
#pragma unroll
      for (int k = 0; k < 8; ++k) a[k] += __shfl_xor(a[k], 32);
      if (lane < 32) {
#pragma unroll
        for (int k = 0; k < 8; ++k) sm[wave * 256 + cc * 8 + k] = a[k];
      }
      __syncthreads();
      if (tid < 256) {
        float s = 0.f;
#pragma unroll
        for (int w = 0; w < AW; ++w) s += sm[w * 256 + tid];
        s_q[tid] = f2bf(s);
      }
      __syncthreads();
    }
    bf16x8 qb[4];
#pragma unroll
    for (int j = 0; j < 4; ++j) qb[j] = __builtin_bit_cast(bf16x8, *reinterpret_cast<const u32x4*>(&s_q[(c & 1) * DH + 32 * j + 8 * g]));

    float m = -INFINITY, l = 0.f;
    f32x2 accA[4], accB[4];
#pragma unroll
    for (int k = 0; k < 4; ++k) { accA[k] = f32x2{0.f, 0.f}; accB[k] = f32x2{0.f, 0.f}; }
    for (int it = 0; it < TILES_PER_WAVE; ++it) {
      u32x4 Kc[4], Vc[4];
#pragma unroll
      for (int j = 0; j < 4; ++j) { Kc[j] = Kn[j]; Vc[j] = Vn[j]; }
      if (it + 1 < TILES_PER_WAVE) ISSUE(wave + AW * (it + 1));
      f32x4 s = {0.f, 0.f, 0.f, 0.f};
#pragma unroll
      for (int j = 0; j < 4; ++j) s = __builtin_amdgcn_mfma_f32_16x16x32_bf16(__builtin_bit_cast(bf16x8, Kc[j]), qb[j], s, 0, 0, 0);
      const float pm = fmaxf(fmaxf(s[0], s[1]), fmaxf(s[2], s[3])) * C2;
      if (__builtin_amdgcn_ballot_w64(pm > m)) {
        const float mn = fmaxf(m, pm);
        const float al = __builtin_amdgcn_exp2f(m - mn);
        const float alo = dpp<XOR1>(al);
        m = mn; l *= al;
#pragma unroll
        for (int k = 0; k < 4; ++k) { accA[k] *= al; accB[k] *= alo; }
      }
      float p[4], po[4];
#pragma unroll
      for (int i = 0; i < 4; ++i) { p[i] = __builtin_amdgcn_exp2f(fmaf(s[i], C2, -m)); po[i] = dpp<XOR1>(p[i]); }
      l += (p[0] + p[1]) + (p[2] + p[3]);
#pragma unroll
      for (int i = 0; i < 4; ++i) {
#pragma unroll
        for (int k = 0; k < 4; ++k) {
          const f32x2 v = {bf_lo(Vc[i][k]), bf_hi(Vc[i][k])};
          accA[k] = __builtin_elementwise_fma(f32x2{p[i], p[i]}, v, accA[k]);
          accB[k] = __builtin_elementwise_fma(f32x2{po[i], po[i]}, v, accB[k]);
        }
      }
    }
#undef ISSUE
    float (*s_acc)[2][DH] = reinterpret_cast<float (*)[2][DH]>(sm);
    float (*s_ml)[2][2] = reinterpret_cast<float (*)[2][2]>(sm + AW * 4 * 2 * DH);
    {
      const int st = wave * 4 + g, hA = c & 1, hB = hA ^ 1;
      *reinterpret_cast<f32x4*>(&s_acc[st][hA][8 * c]) = f32x4{accA[0][0], accA[0][1], accA[1][0], accA[1][1]};
      *reinterpret_cast<f32x4*>(&s_acc[st][hA][8 * c + 4]) = f32x4{accA[2][0], accA[2][1], accA[3][0], accA[3][1]};
      *reinterpret_cast<f32x4*>(&s_acc[st][hB][8 * c]) = f32x4{accB[0][0], accB[0][1], accB[1][0], accB[1][1]};
      *reinterpret_cast<f32x4*>(&s_acc[st][hB][8 * c + 4]) = f32x4{accB[2][0], accB[2][1], accB[3][0], accB[3][1]};
      if (c < 2) { s_ml[st][c][0] = m; s_ml[st][c][1] = l; }
    }
    __syncthreads();
    if (tid < 256) {
      const int h = tid >> 7, d = tid & 127;
      float M = -INFINITY;
#pragma unroll
      for (int s2 = 0; s2 < AW * 4; ++s2) M = fmaxf(M, s_ml[s2][h][0]);
      float L = 0.f, o = 0.f;
#pragma unroll
      for (int s2 = 0; s2 < AW * 4; ++s2) {
        const float w = __builtin_amdgcn_exp2f(s_ml[s2][h][0] - M);
        L = fmaf(w, s_ml[s2][h][1], L); o = fmaf(w, s_acc[s2][h][d], o);
      }
      const size_t slot = ((size_t)(pair * 2 + h)) * NSPLIT + split;
      st_agent(reinterpret_cast<float*>(ws + WS_PO) + slot * DH + d, o);
      if (d == 0) { float* ml = reinterpret_cast<float*>(ws + WS_ML) + slot * 2; st_agent(ml, M); st_agent(ml + 1, L); }
    }
    asm volatile("s_waitcnt vmcnt(0)" ::: "memory");
    __syncthreads();
    if (tid == 0) {
      const unsigned old = __hip_atomic_fetch_add(&g_ticket[pair], 1u, __ATOMIC_RELAXED, __HIP_MEMORY_SCOPE_AGENT);
      const unsigned last = (old % NSPLIT == NSPLIT - 1) ? 1u : 0u;
      if (last) __builtin_amdgcn_fence(__ATOMIC_ACQUIRE, "agent");
      s_flag = last;
    }
    __syncthreads();
    if (s_flag == 0u) return;
    {
      const int cc = tid % 72, rg = tid / 72;
      const unsigned short* wp = wo + (size_t)(kvh * 256 + rg) * DM + cc * 8;
      u32x4 wv[10];
      if (rg < 7) {
#pragma unroll
        for (int i = 0; i < 10; ++i) wv[i] = *reinterpret_cast<const u32x4*>(wp + (size_t)(7 * i) * DM);
      }
      if (tid < 256) {
        const int h = tid >> 7, d = tid & 127;
        const size_t row = (size_t)(pair * 2 + h);
        const float* ml = reinterpret_cast<const float*>(ws + WS_ML) + row * NSPLIT * 2;
        const float* po = reinterpret_cast<const float*>(ws + WS_PO) + row * NSPLIT * DH + d;
        float M = -INFINITY;
#pragma unroll
        for (int s = 0; s < NSPLIT; ++s) M = fmaxf(M, ml[2 * s]);
        float L = 0.f, o = 0.f;
#pragma unroll
        for (int s = 0; s < NSPLIT; ++s) { const float wgt = __builtin_amdgcn_exp2f(ml[2 * s] - M); L = fmaf(wgt, ml[2 * s + 1], L); o = fmaf(wgt, po[(size_t)s * DH], o); }
        xs[0][tid] = rbf(o / L);
      }
      __syncthreads();
      float a[8];
#pragma unroll
      for (int k = 0; k < 8; ++k) a[k] = 0.f;
      if (rg < 7) {
#pragma unroll
        for (int i = 0; i < 10; ++i) { const float xv = xs[0][rg + 7 * i]; FMA8(a, xv, wv[i]); }
#pragma unroll 1
        for (int bt = 1; bt < 4; ++bt) {
#pragma unroll
          for (int i = 0; i < 9; ++i) { const int r = rg + 7 * (1 + 9 * bt + i); if (r < 256) wv[i] = *reinterpret_cast<const u32x4*>(wp + (size_t)(r - rg) * DM); }
#pragma unroll
          for (int i = 0; i < 9; ++i) { const int r = rg + 7 * (1 + 9 * bt + i); if (r < 256) { const float xv = xs[0][r]; FMA8(a, xv, wv[i]); } }
        }
#pragma unroll
        for (int k = 0; k < 8; ++k) sm[rg * DM + cc * 8 + k] = a[k];
      }
      __syncthreads();
      for (int i = tid; i < DM; i += NTHR) {
        float s = 0.f;
#pragma unroll
        for (int r = 0; r < 7; ++r) s += sm[r * DM + i];
        st_agent(reinterpret_cast<float*>(ws + WS_PA) + (size_t)pair * DM + i, s);
      }
    }
  } else {
    const int f0 = (id - NATT) * 128;
    const int cc = tid & 15, rg = tid >> 4;
    for (int i = tid; i < 2 * DM; i += NTHR) xs[i / DM][i % DM] = bf2f(x[i]);
    float ag[2][8], au[2][8];
#pragma unroll
    for (int k = 0; k < 8; ++k) { ag[0][k] = ag[1][k] = au[0][k] = au[1][k] = 0.f; }
    __syncthreads();
#pragma unroll 1
    for (int bt = 0; bt < 4; ++bt) {
      u32x4 wv[5], wv2[5];
#pragma unroll
      for (int i = 0; i < 5; ++i) if (5 * bt + i < 18) {
        wv[i] = *reinterpret_cast<const u32x4*>(wg + (size_t)(rg + 32 * (5 * bt + i)) * DF + f0 + cc * 8);
        wv2[i] = *reinterpret_cast<const u32x4*>(wu + (size_t)(rg + 32 * (5 * bt + i)) * DF + f0 + cc * 8);
      }
#pragma unroll
      for (int i = 0; i < 5; ++i) if (5 * bt + i < 18) {
        const float x0 = xs[0][rg + 32 * (5 * bt + i)], x1 = xs[1][rg + 32 * (5 * bt + i)];
        FMA8(ag[0], x0, wv[i]); FMA8(ag[1], x1, wv[i]); FMA8(au[0], x0, wv2[i]); FMA8(au[1], x1, wv2[i]);
      }
    }
#pragma unroll
    for (int k = 0; k < 8; ++k) {
#pragma unroll
      for (int o = 16; o < 64; o <<= 1) {
        ag[0][k] += __shfl_xor(ag[0][k], o); ag[1][k] += __shfl_xor(ag[1][k], o);
        au[0][k] += __shfl_xor(au[0][k], o); au[1][k] += __shfl_xor(au[1][k], o);
      }
    }
    const int dc = tid % 72, dr = tid / 72;
    if (lane < 16) {
#pragma unroll
      for (int k = 0; k < 8; ++k) {
        sm[(wave * 4 + 0) * 128 + cc * 8 + k] = ag[0][k]; sm[(wave * 4 + 1) * 128 + cc * 8 + k] = ag[1][k];
        sm[(wave * 4 + 2) * 128 + cc * 8 + k] = au[0][k]; sm[(wave * 4 + 3) * 128 + cc * 8 + k] = au[1][k];
      }
    }
    u32x4 dv[8];
    if (dr < 4) {
#pragma unroll
      for (int i = 0; i < 8; ++i) dv[i] = *reinterpret_cast<const u32x4*>(wd + (size_t)(f0 + dr + 4 * i) * DM + dc * 8);
    }
    __syncthreads();
    if (tid < 256) {
      const int b = tid >> 7, f = tid & 127;
      float gs = 0.f, us = 0.f;
#pragma unroll
      for (int w = 0; w < AW; ++w) { gs += sm[(w * 4 + b) * 128 + f]; us += sm[(w * 4 + 2 + b) * 128 + f]; }
      gs = rbf(gs); us = rbf(us);
      const float t = tanhf(0.7978845608028654f * (gs + 0.044715f * gs * gs * gs));
      xs[b][f] = rbf(rbf(0.5f * gs * (1.f + t)) * us);
    }
    __syncthreads();
    float a0[8], a1[8];
#pragma unroll
    for (int k = 0; k < 8; ++k) { a0[k] = 0.f; a1[k] = 0.f; }
    if (dr < 4) {
#pragma unroll
      for (int i = 0; i < 8; ++i) { const float h0 = xs[0][dr + 4 * i], h1 = xs[1][dr + 4 * i]; FMA8(a0, h0, dv[i]); FMA8(a1, h1, dv[i]); }
#pragma unroll 1
      for (int bt = 1; bt < 4; ++bt) {
#pragma unroll
        for (int i = 0; i < 8; ++i) dv[i] = *reinterpret_cast<const u32x4*>(wd + (size_t)(f0 + dr + 4 * (8 * bt + i)) * DM + dc * 8);
#pragma unroll
        for (int i = 0; i < 8; ++i) { const float h0 = xs[0][dr + 4 * (8 * bt + i)], h1 = xs[1][dr + 4 * (8 * bt + i)]; FMA8(a0, h0, dv[i]); FMA8(a1, h1, dv[i]); }
      }
#pragma unroll
      for (int k = 0; k < 8; ++k) { sm[(dr * 2 + 0) * DM + dc * 8 + k] = a0[k]; sm[(dr * 2 + 1) * DM + dc * 8 + k] = a1[k]; }
    }
    __syncthreads();
    for (int i = tid; i < 2 * DM; i += NTHR) {
      const int b = i / DM, c = i % DM;
      const float s = (sm[(0 + b) * DM + c] + sm[(2 + b) * DM + c]) + (sm[(4 + b) * DM + c] + sm[(6 + b) * DM + c]);
      st_agent(reinterpret_cast<float*>(ws + WS_PB) + (size_t)(id - NATT) * 2 * DM + i, s);
    }
  }
  asm volatile("s_waitcnt vmcnt(0)" ::: "memory");
  __syncthreads();
  if (tid == 0) {
    const unsigned old = __hip_atomic_fetch_add(&g_ticket[32], 1u, __ATOMIC_RELAXED, __HIP_MEMORY_SCOPE_AGENT);
    const unsigned last = (old % NARRIVE == NARRIVE - 1) ? 1u : 0u;
    if (last) __builtin_amdgcn_fence(__ATOMIC_ACQUIRE, "agent");
    s_flag = last;
  }
  __syncthreads();
  if (s_flag == 0u) return;
  {
    float zt[2] = {0.f, 0.f};
    for (int i = tid; i < DM; i += NTHR) {
      const float gv = bf2f(gw[i]);
      zt[0] += rbf(bf2f(x[i]) * gv); zt[1] += rbf(bf2f(x[DM + i]) * gv);
    }
#pragma unroll
    for (int o = 1; o < 64; o <<= 1) { zt[0] += __shfl_xor(zt[0], o); zt[1] += __shfl_xor(zt[1], o); }
    if (lane == 0) { sm[wave * 2] = zt[0]; sm[wave * 2 + 1] = zt[1]; }
    __syncthreads();
    for (int i = tid; i < 2 * DM; i += NTHR) {
      const int b = i / DM, c = i % DM;
      const float* pa = reinterpret_cast<const float*>(ws + WS_PA) + (size_t)b * HKV * DM + c;
      const float* pb = reinterpret_cast<const float*>(ws + WS_PB) + i;
      float a = 0.f, bb = 0.f;
#pragma unroll
      for (int s = 0; s < HKV; ++s) a += pa[(size_t)s * DM];
#pragma unroll
      for (int s = 0; s < NMLP; ++s) bb += pb[(size_t)s * 2 * DM];
      float z = 0.f;
#pragma unroll
      for (int w = 0; w < AW; ++w) z += sm[w * 2 + b];
      z = rbf(z);
      const float gt = rbf(1.f / (1.f + __expf(-z)));
      a = rbf(a); bb = rbf(bb);
      out[i] = f2bf(rbf(rbf(gt * a) + rbf(rbf(1.f - gt) * bb)));
    }
  }
}

}

extern "C" void kernel_launch(void* const* d_in, const int* in_sizes, int n_in, void* d_out, int out_size, void* d_ws,
                              size_t ws_size, hipStream_t stream) {
  k_fused<<<NATT + NMLP, NTHR, 0, stream>>>((const unsigned short*)d_in[0], (const unsigned short*)d_in[1],
                                            (const unsigned short*)d_in[2], (const unsigned short*)d_in[3],
                                            (const unsigned short*)d_in[4], (const unsigned short*)d_in[6],
                                            (const unsigned short*)d_in[7], (const unsigned short*)d_in[8],
                                            (const unsigned short*)d_in[9], (unsigned short*)d_out, (unsigned char*)d_ws);
}
```

```cpp
#include <hip/hip_runtime.h>
#include <hip/hip_bf16.h>
#include <cstdint>

namespace {

constexpr int BATCH = 2, DM = 576, HQ = 32, HKV = 16, SK = 32768, DH = 128, DF = 4608;
constexpr int NQ = HQ * DH;
constexpr int NSPLIT = 8, AW = 8, NTHR = AW * 64;
constexpr int KEYS_PER_WG = SK / NSPLIT;
constexpr int TILES_PER_WAVE = KEYS_PER_WG / 16 / AW;
constexpr int NATT = BATCH * HKV * NSPLIT;
constexpr int NMLP = DF / 128;
constexpr unsigned NGROUPS = BATCH * HKV;
constexpr unsigned NARRIVE = NGROUPS + NMLP;

constexpr size_t WS_Q = 0;
constexpr size_t WS_ML = WS_Q + (size_t)BATCH * NQ * 2;
constexpr size_t WS_PO = WS_ML + (size_t)BATCH * HQ * NSPLIT * 2 * 4;
constexpr int NPOST = 64;
constexpr size_t WS_PA = WS_PO + (size_t)BATCH * HQ * NSPLIT * DH * 4;
constexpr size_t WS_PB = WS_PA + (size_t)NPOST * 2 * DM * 4;

__device__ unsigned g_ticket;

using bf16x8 = __attribute__((ext_vector_type(8))) short;
using f32x4 = __attribute__((ext_vector_type(4))) float;
using f32x2 = __attribute__((ext_vector_type(2))) float;
using u32x4 = __attribute__((ext_vector_type(4))) unsigned;

__device__ __forceinline__ float bf_lo(unsigned u) { return __uint_as_float(u << 16); }
__device__ __forceinline__ float bf_hi(unsigned u) { return __uint_as_float(u & 0xffff0000u); }
__device__ __forceinline__ float bf2f(unsigned short h) { return __uint_as_float((unsigned)h << 16); }
__device__ __forceinline__ unsigned short f2bf(float f) {
  unsigned u = __float_as_uint(f);
  u += 0x7fffu + ((u >> 16) & 1u);
  return (unsigned short)(u >> 16);
}
__device__ __forceinline__ float rbf(float f) { return bf2f(f2bf(f)); }
template <int CTRL> __device__ __forceinline__ float dpp(float x) {
  return __builtin_bit_cast(float, __builtin_amdgcn_mov_dpp(__builtin_bit_cast(int, x), CTRL, 0xf, 0xf, true));
}
constexpr int XOR1 = 0xB1;
__device__ __forceinline__ void st_agent(float* p, float v) { __hip_atomic_store(p, v, __ATOMIC_RELAXED, __HIP_MEMORY_SCOPE_AGENT); }
#define FMA8(A_, X_, W_) do { _Pragma("unroll") for (int k_ = 0; k_ < 4; ++k_) { \
    A_[2 * k_] = fmaf((X_), bf_lo((W_)[k_]), A_[2 * k_]); A_[2 * k_ + 1] = fmaf((X_), bf_hi((W_)[k_]), A_[2 * k_ + 1]); } } while (0)

constexpr int SM_FLOATS = AW * 4 * 2 * DH + 256;

__global__ void __launch_bounds__(256) k_q(const unsigned short* __restrict__ x, const unsigned short* __restrict__ wq,
                                           unsigned char* __restrict__ ws) {
  __shared__ float xs[2][DM];
  __shared__ float red[4][2][32];
  const int tid = threadIdx.x, lane = tid & 63, wave = tid >> 6;
  const int col0 = blockIdx.x * 32, cc = tid & 3, rg = tid >> 2;
  u32x4 wv[9];
#pragma unroll
  for (int i = 0; i < 9; ++i) wv[i] = *reinterpret_cast<const u32x4*>(wq + (size_t)(rg + 64 * i) * NQ + col0 + cc * 8);
  for (int i = tid; i < 2 * DM; i += 256) xs[i / DM][i % DM] = bf2f(x[i]);
  __syncthreads();
  float a0[8], a1[8];
#pragma unroll
  for (int k = 0; k < 8; ++k) { a0[k] = 0.f; a1[k] = 0.f; }
#pragma unroll
  for (int i = 0; i < 9; ++i) { const float x0 = xs[0][rg + 64 * i], x1 = xs[1][rg + 64 * i]; FMA8(a0, x0, wv[i]); FMA8(a1, x1, wv[i]); }
#pragma unroll
  for (int k = 0; k < 8; ++k) {
#pragma unroll
    for (int o = 4; o < 64; o <<= 1) { a0[k] += __shfl_xor(a0[k], o); a1[k] += __shfl_xor(a1[k], o); }
  }
  if (lane < 4) {
#pragma unroll
    for (int k = 0; k < 8; ++k) { red[wave][0][lane * 8 + k] = a0[k]; red[wave][1][lane * 8 + k] = a1[k]; }
  }
  __syncthreads();
  if (tid < 64) {
    const int b = tid >> 5, c = tid & 31;
    reinterpret_cast<unsigned short*>(ws + WS_Q)[b * NQ + col0 + c] = f2bf((red[0][b][c] + red[1][b][c]) + (red[2][b][c] + red[3][b][c]));
  }
}

__global__ void __launch_bounds__(NTHR, 4) k_attn(const unsigned short* __restrict__ x, const unsigned short* __restrict__ kc,
                                                  const unsigned short* __restrict__ vc, const unsigned short* __restrict__ wg,
                                                  const unsigned short* __restrict__ wu, const unsigned short* __restrict__ wd,
                                                  unsigned char* __restrict__ ws) {
  __shared__ __attribute__((aligned(16))) float sm[SM_FLOATS];
  __shared__ float xs[2][DM];
  const int tid = threadIdx.x, lane = tid & 63, wave = __builtin_amdgcn_readfirstlane(tid >> 6);
  const int id = blockIdx.x;
  if (id < NATT) {
    const int pair = (id & 7) + 8 * ((id >> 3) & 3), split = id >> 5;
    const int b = pair >> 4, kvh = pair & 15;
    const int g = lane >> 4, c = lane & 15;
    constexpr float C2 = 0.088388347648318440f * 1.4426950408889634f;
    const size_t head_off = ((size_t)pair * SK + (size_t)split * KEYS_PER_WG) * DH;
    const unsigned short* kp = kc + head_off + (size_t)c * DH + 8 * g;
    const unsigned short* vp = vc + head_off + (size_t)(4 * g) * DH + 8 * c;
    u32x4 Kn[4], Vn[4];
#define ISSUE(t_) do { const size_t o_ = (size_t)(t_) * 16 * DH; \
    _Pragma("unroll") for (int j_ = 0; j_ < 4; ++j_) Kn[j_] = __builtin_nontemporal_load(reinterpret_cast<const u32x4*>(kp + o_ + 32 * j_)); \
    _Pragma("unroll") for (int j_ = 0; j_ < 4; ++j_) Vn[j_] = __builtin_nontemporal_load(reinterpret_cast<const u32x4*>(vp + o_ + (size_t)j_ * DH)); } while (0)
    ISSUE(wave);
    bf16x8 qb[4];
    {
      const unsigned short* q = reinterpret_cast<const unsigned short*>(ws + WS_Q) + (size_t)b * NQ + (size_t)(kvh * 2 + (c & 1)) * DH;
#pragma unroll
      for (int j = 0; j < 4; ++j) qb[j] = __builtin_bit_cast(bf16x8, *reinterpret_cast<const u32x4*>(q + 32 * j + 8 * g));
    }
    float m = -INFINITY, l = 0.f;
    f32x2 accA[4], accB[4];
#pragma unroll
    for (int k = 0; k < 4; ++k) { accA[k] = f32x2{0.f, 0.f}; accB[k] = f32x2{0.f, 0.f}; }
    for (int it = 0; it < TILES_PER_WAVE; ++it) {
      u32x4 Kc[4], Vc[4];
#pragma unroll
      for (int j = 0; j < 4; ++j) { Kc[j] = Kn[j]; Vc[j] = Vn[j]; }
      if (it + 1 < TILES_PER_WAVE) ISSUE(wave + AW * (it + 1));
      f32x4 s = {0.f, 0.f, 0.f, 0.f};
#pragma unroll
      for (int j = 0; j < 4; ++j) s = __builtin_amdgcn_mfma_f32_16x16x32_bf16(__builtin_bit_cast(bf16x8, Kc[j]), qb[j], s, 0, 0, 0);
      const float pm = fmaxf(fmaxf(s[0], s[1]), fmaxf(s[2], s[3])) * C2;
      if (__builtin_amdgcn_ballot_w64(pm > m)) {
        const float mn = fmaxf(m, pm);
        const float al = __builtin_amdgcn_exp2f(m - mn);
        const float alo = dpp<XOR1>(al);
        m = mn; l *= al;
#pragma unroll
        for (int k = 0; k < 4; ++k) { accA[k] *= al; accB[k] *= alo; }
      }
      float p[4], po[4];
#pragma unroll
      for (int i = 0; i < 4; ++i) { p[i] = __builtin_amdgcn_exp2f(fmaf(s[i], C2, -m)); po[i] = dpp<XOR1>(p[i]); }
      l += (p[0] + p[1]) + (p[2] + p[3]);
#pragma unroll
      for (int i = 0; i < 4; ++i) {
#pragma unroll
        for (int k = 0; k < 4; ++k) {
          const f32x2 v = {bf_lo(Vc[i][k]), bf_hi(Vc[i][k])};
          accA[k] = __builtin_elementwise_fma(f32x2{p[i], p[i]}, v, accA[k]);
          accB[k] = __builtin_elementwise_fma(f32x2{po[i], po[i]}, v, accB[k]);
        }
      }
    }
#undef ISSUE
    float (*s_acc)[2][DH] = reinterpret_cast<float (*)[2][DH]>(sm);
    float (*s_ml)[2][2] = reinterpret_cast<float (*)[2][2]>(sm + AW * 4 * 2 * DH);
    {
      const int st = wave * 4 + g, hA = c & 1, hB = hA ^ 1;
      *reinterpret_cast<f32x4*>(&s_acc[st][hA][8 * c]) = f32x4{accA[0][0], accA[0][1], accA[1][0], accA[1][1]};
      *reinterpret_cast<f32x4*>(&s_acc[st][hA][8 * c + 4]) = f32x4{accA[2][0], accA[2][1], accA[3][0], accA[3][1]};
      *reinterpret_cast<f32x4*>(&s_acc[st][hB][8 * c]) = f32x4{accB[0][0], accB[0][1], accB[1][0], accB[1][1]};
      *reinterpret_cast<f32x4*>(&s_acc[st][hB][8 * c + 4]) = f32x4{accB[2][0], accB[2][1], accB[3][0], accB[3][1]};
      if (c < 2) { s_ml[st][c][0] = m; s_ml[st][c][1] = l; }
    }
    __syncthreads();
    if (tid < 256) {
      const int h = tid >> 7, d = tid & 127;
      float M = -INFINITY;
#pragma unroll
      for (int s2 = 0; s2 < AW * 4; ++s2) M = fmaxf(M, s_ml[s2][h][0]);
      float L = 0.f, o = 0.f;
#pragma unroll
      for (int s2 = 0; s2 < AW * 4; ++s2) {
        const float w = __builtin_amdgcn_exp2f(s_ml[s2][h][0] - M);
        L = fmaf(w, s_ml[s2][h][1], L); o = fmaf(w, s_acc[s2][h][d], o);
      }
      const size_t slot = ((size_t)(pair * 2 + h)) * NSPLIT + split;
      reinterpret_cast<float*>(ws + WS_PO)[slot * DH + d] = o;
      if (d == 0) { float* ml = reinterpret_cast<float*>(ws + WS_ML) + slot * 2; ml[0] = M; ml[1] = L; }
    }
  } else {
    const int f0 = (id - NATT) * 128;
    const int cc = tid & 15, rg = tid >> 4;
    for (int i = tid; i < 2 * DM; i += NTHR) xs[i / DM][i % DM] = bf2f(x[i]);
    float ag[2][8], au[2][8];
#pragma unroll
    for (int k = 0; k < 8; ++k) { ag[0][k] = ag[1][k] = au[0][k] = au[1][k] = 0.f; }
    __syncthreads();
#pragma unroll 1
    for (int bt = 0; bt < 4; ++bt) {
      u32x4 wv[5], wv2[5];
#pragma unroll
      for (int i = 0; i < 5; ++i) if (5 * bt + i < 18) {
        wv[i] = *reinterpret_cast<const u32x4*>(wg + (size_t)(rg + 32 * (5 * bt + i)) * DF + f0 + cc * 8);
        wv2[i] = *reinterpret_cast<const u32x4*>(wu + (size_t)(rg + 32 * (5 * bt + i)) * DF + f0 + cc * 8);
      }
#pragma unroll
      for (int i = 0; i < 5; ++i) if (5 * bt + i < 18) {
        const float x0 = xs[0][rg + 32 * (5 * bt + i)], x1 = xs[1][rg + 32 * (5 * bt + i)];
        FMA8(ag[0], x0, wv[i]); FMA8(ag[1], x1, wv[i]); FMA8(au[0], x0, wv2[i]); FMA8(au[1], x1, wv2[i]);
      }
    }
#pragma unroll
    for (int k = 0; k < 8; ++k) {
#pragma unroll
      for (int o = 16; o < 64; o <<= 1) {
        ag[0][k] += __shfl_xor(ag[0][k], o); ag[1][k] += __shfl_xor(ag[1][k], o);
        au[0][k] += __shfl_xor(au[0][k], o); au[1][k] += __shfl_xor(au[1][k], o);
      }
    }
    const int dc = tid % 72, dr = tid / 72;
    if (lane < 16) {
#pragma unroll
      for (int k = 0; k < 8; ++k) {
        sm[(wave * 4 + 0) * 128 + cc * 8 + k] = ag[0][k]; sm[(wave * 4 + 1) * 128 + cc * 8 + k] = ag[1][k];
        sm[(wave * 4 + 2) * 128 + cc * 8 + k] = au[0][k]; sm[(wave * 4 + 3) * 128 + cc * 8 + k] = au[1][k];
      }
    }
    u32x4 dv[8];
    if (dr < 4) {
#pragma unroll
      for (int i = 0; i < 8; ++i) dv[i] = *reinterpret_cast<const u32x4*>(wd + (size_t)(f0 + dr + 4 * i) * DM + dc * 8);
    }
    __syncthreads();
    if (tid < 256) {
      const int b = tid >> 7, f = tid & 127;
      float gs = 0.f, us = 0.f;
#pragma unroll
      for (int w = 0; w < AW; ++w) { gs += sm[(w * 4 + b) * 128 + f]; us += sm[(w * 4 + 2 + b) * 128 + f]; }
      gs = rbf(gs); us = rbf(us);
      const float t = tanhf(0.7978845608028654f * (gs + 0.044715f * gs * gs * gs));
      xs[b][f] = rbf(rbf(0.5f * gs * (1.f + t)) * us);
    }
    __syncthreads();
    float a0[8], a1[8];
#pragma unroll
    for (int k = 0; k < 8; ++k) { a0[k] = 0.f; a1[k] = 0.f; }
    if (dr < 4) {
#pragma unroll
      for (int i = 0; i < 8; ++i) { const float h0 = xs[0][dr + 4 * i], h1 = xs[1][dr + 4 * i]; FMA8(a0, h0, dv[i]); FMA8(a1, h1, dv[i]); }
#pragma unroll 1
      for (int bt = 1; bt < 4; ++bt) {
#pragma unroll
        for (int i = 0; i < 8; ++i) dv[i] = *reinterpret_cast<const u32x4*>(wd + (size_t)(f0 + dr + 4 * (8 * bt + i)) * DM + dc * 8);
#pragma unroll
        for (int i = 0; i < 8; ++i) { const float h0 = xs[0][dr + 4 * (8 * bt + i)], h1 = xs[1][dr + 4 * (8 * bt + i)]; FMA8(a0, h0, dv[i]); FMA8(a1, h1, dv[i]); }
      }
#pragma unroll
      for (int k = 0; k < 8; ++k) { sm[(dr * 2 + 0) * DM + dc * 8 + k] = a0[k]; sm[(dr * 2 + 1) * DM + dc * 8 + k] = a1[k]; }
    }
    __syncthreads();
    for (int i = tid; i < 2 * DM; i += NTHR) {
      const int b = i / DM, c = i % DM;
      const float s = (sm[(0 + b) * DM + c] + sm[(2 + b) * DM + c]) + (sm[(4 + b) * DM + c] + sm[(6 + b) * DM + c]);
      reinterpret_cast<float*>(ws + WS_PB)[(size_t)(id - NATT) * 2 * DM + i] = s;
    }
  }
}

__global__ void __launch_bounds__(576) k_post(const unsigned short* __restrict__ x, const unsigned short* __restrict__ wo,
                                              const unsigned short* __restrict__ gw, unsigned short* __restrict__ out,
                                              unsigned char* __restrict__ ws) {
  __shared__ float vin[2][64];
  __shared__ float red[8][2][DM];
  __shared__ unsigned s_flag;
  const int tid = threadIdx.x, lane = tid & 63, wave = tid >> 6, blk = blockIdx.x;
  const int h = blk >> 1, hf = blk & 1;
  const int cc = tid % 72, rg = tid / 72;
  u32x4 wv[8];
#pragma unroll
  for (int i = 0; i < 8; ++i) wv[i] = *reinterpret_cast<const u32x4*>(wo + (size_t)(blk * 64 + rg + 8 * i) * DM + cc * 8);
  if (tid < 128) {
    const int b = tid >> 6, d = hf * 64 + (tid & 63);
    const size_t row = (size_t)b * HQ + h;
    const float* ml = reinterpret_cast<const float*>(ws + WS_ML) + row * NSPLIT * 2;
    const float* po = reinterpret_cast<const float*>(ws + WS_PO) + row * NSPLIT * DH + d;
    float mv[NSPLIT], lv[NSPLIT], pv[NSPLIT];
#pragma unroll
    for (int s = 0; s < NSPLIT; ++s) { mv[s] = ml[2 * s]; lv[s] = ml[2 * s + 1]; pv[s] = po[(size_t)s * DH]; }
    float M = -INFINITY;
#pragma unroll
    for (int s = 0; s < NSPLIT; ++s) M = fmaxf(M, mv[s]);
    float L = 0.f, o = 0.f;
#pragma unroll
    for (int s = 0; s < NSPLIT; ++s) { const float wgt = __builtin_amdgcn_exp2f(mv[s] - M); L = fmaf(wgt, lv[s], L); o = fmaf(wgt, pv[s], o); }
    vin[b][tid & 63] = rbf(o / L);
  }
  __syncthreads();
  float a0[8], a1[8];
#pragma unroll
  for (int k = 0; k < 8; ++k) { a0[k] = 0.f; a1[k] = 0.f; }
#pragma unroll
  for (int i = 0; i < 8; ++i) { const float x0 = vin[0][rg + 8 * i], x1 = vin[1][rg + 8 * i]; FMA8(a0, x0, wv[i]); FMA8(a1, x1, wv[i]); }
#pragma unroll
  for (int k = 0; k < 8; ++k) { red[rg][0][cc * 8 + k] = a0[k]; red[rg][1][cc * 8 + k] = a1[k]; }
  __syncthreads();
  float* part = reinterpret_cast<float*>(ws + WS_PA) + (size_t)blk * 2 * DM;
  for (int i = tid; i < 2 * DM; i += 576) {
    const int b = i / DM, c = i % DM;
    float s = 0.f;
#pragma unroll
    for (int r = 0; r < 8; ++r) s += red[r][b][c];
    st_agent(part + i, s);
  }
  asm volatile("s_waitcnt vmcnt(0)" ::: "memory");
  __syncthreads();
  if (tid == 0) {
    const unsigned old = __hip_atomic_fetch_add(&g_ticket, 1u, __ATOMIC_RELAXED, __HIP_MEMORY_SCOPE_AGENT);
    const unsigned last = (old % NPOST == NPOST - 1) ? 1u : 0u;
    if (last) __builtin_amdgcn_fence(__ATOMIC_ACQUIRE, "agent");
    s_flag = last;
  }
  __syncthreads();
  if (s_flag == 0u) return;
  float zt0 = rbf(bf2f(x[tid]) * bf2f(gw[tid])), zt1 = rbf(bf2f(x[DM + tid]) * bf2f(gw[tid]));
  float av[2], bv[2];
#pragma unroll
  for (int b = 0; b < 2; ++b) {
    const float* pa = reinterpret_cast<const float*>(ws + WS_PA) + (size_t)b * DM + tid;
    const float* pb = reinterpret_cast<const float*>(ws + WS_PB) + (size_t)b * DM + tid;
    float a = 0.f, bb = 0.f;
#pragma unroll 1
    for (int s0 = 0; s0 < NPOST; s0 += 16) {
      float t[16];
#pragma unroll
      for (int s = 0; s < 16; ++s) t[s] = pa[(size_t)(s0 + s) * 2 * DM];
#pragma unroll
      for (int s = 0; s < 16; ++s) a += t[s];
    }
#pragma unroll 1
    for (int s0 = 0; s0 < NMLP; s0 += 12) {
      float t[12];
#pragma unroll
      for (int s = 0; s < 12; ++s) t[s] = pb[(size_t)(s0 + s) * 2 * DM];
#pragma unroll
      for (int s = 0; s < 12; ++s) bb += t[s];
    }
    av[b] = rbf(a); bv[b] = rbf(bb);
  }
#pragma unroll
  for (int o = 1; o < 64; o <<= 1) { zt0 += __shfl_xor(zt0, o); zt1 += __shfl_xor(zt1, o); }
  if (lane == 0) { vin[0][wave] = zt0; vin[1][wave] = zt1; }
  __syncthreads();
#pragma unroll
  for (int b = 0; b < 2; ++b) {
    float z = 0.f;
#pragma unroll
    for (int w = 0; w < 9; ++w) z += vin[b][w];
    z = rbf(z);
    const float gt = rbf(1.f / (1.f + __expf(-z)));
    out[b * DM + tid] = f2bf(rbf(rbf(gt * av[b]) + rbf(rbf(1.f - gt) * bv[b])));
  }
}

}

extern "C" void kernel_launch(void* const* d_in, const int* in_sizes, int n_in, void* d_out, int out_size, void* d_ws,
                              size_t ws_size, hipStream_t stream) {
  const unsigned short* x = (const unsigned short*)d_in[0];
  unsigned char* ws = (unsigned char*)d_ws;
  k_q<<<NQ / 32, 256, 0, stream>>>(x, (const unsigned short*)d_in[3], ws);
  k_attn<<<NATT + NMLP, NTHR, 0, stream>>>(x, (const unsigned short*)d_in[1], (const unsigned short*)d_in[2],
                                           (const unsigned short*)d_in[6], (const unsigned short*)d_in[7],
                                           (const unsigned short*)d_in[8], ws);
  k_post<<<NPOST, 576, 0, stream>>>(x, (const unsigned short*)d_in[4], (const unsigned short*)d_in[9], (unsigned short*)d_out, ws);
}
```

```cpp
#include <hip/hip_runtime.h>
#include <hip/hip_bf16.h>
#include <cstdint>

namespace {

constexpr int BATCH = 2, DM = 576, HQ = 32, HKV = 16, SK = 32768, DH = 128, DF = 4608;
constexpr int NQ = HQ * DH;
constexpr int NSPLIT = 8, AW = 8, NTHR = AW * 64;
constexpr int KEYS_PER_WG = SK / NSPLIT;
constexpr int TILES_PER_WAVE = KEYS_PER_WG / 16 / AW;
constexpr int NATT = BATCH * HKV * NSPLIT;

constexpr size_t WS_Q = 0;
constexpr size_t WS_ML = WS_Q + (size_t)BATCH * NQ * 2;
constexpr size_t WS_PO = WS_ML + (size_t)BATCH * HQ * NSPLIT * 2 * 4;
constexpr int NPART_A = HQ, NPART_B = DF / 128, NPOST = NPART_A + NPART_B;
constexpr size_t WS_GU = WS_PO + (size_t)BATCH * HQ * NSPLIT * DH * 4;
constexpr size_t WS_PART = WS_GU + (size_t)BATCH * 2 * DF * 4;

__device__ unsigned g_ticket;

using bf16x8 = __attribute__((ext_vector_type(8))) short;
using f32x4 = __attribute__((ext_vector_type(4))) float;
using f32x2 = __attribute__((ext_vector_type(2))) float;
using u32x4 = __attribute__((ext_vector_type(4))) unsigned;

__device__ __forceinline__ float bf_lo(unsigned u) { return __uint_as_float(u << 16); }
__device__ __forceinline__ float bf_hi(unsigned u) { return __uint_as_float(u & 0xffff0000u); }
__device__ __forceinline__ float bf2f(unsigned short h) { return __uint_as_float((unsigned)h << 16); }
__device__ __forceinline__ unsigned short f2bf(float f) {
  unsigned u = __float_as_uint(f);
  u += 0x7fffu + ((u >> 16) & 1u);
  return (unsigned short)(u >> 16);
}
__device__ __forceinline__ float rbf(float f) { return bf2f(f2bf(f)); }
template <int CTRL> __device__ __forceinline__ float dpp(float x) {
  return __builtin_bit_cast(float, __builtin_amdgcn_mov_dpp(__builtin_bit_cast(int, x), CTRL, 0xf, 0xf, true));
}
constexpr int XOR1 = 0xB1;
__device__ __forceinline__ void st_agent(float* p, float v) { __hip_atomic_store(p, v, __ATOMIC_RELAXED, __HIP_MEMORY_SCOPE_AGENT); }
#define FMA8(A_, X_, W_) do { _Pragma("unroll") for (int k_ = 0; k_ < 4; ++k_) { \
    A_[2 * k_] = fmaf((X_), bf_lo((W_)[k_]), A_[2 * k_]); A_[2 * k_ + 1] = fmaf((X_), bf_hi((W_)[k_]), A_[2 * k_ + 1]); } } while (0)

constexpr int SM_FLOATS = AW * 4 * 2 * DH + 256;

__global__ void __launch_bounds__(256) k_q(const unsigned short* __restrict__ x, const unsigned short* __restrict__ wq,
                                           const unsigned short* __restrict__ wg, const unsigned short* __restrict__ wu,
                                           unsigned char* __restrict__ ws) {
  __shared__ float xs[2][DM];
  __shared__ float red[4][2][32];
  const int tid = threadIdx.x, lane = tid & 63, wave = tid >> 6;
  const int colg = blockIdx.x * 32, cc = tid & 3, rg = tid >> 2;
  const unsigned short* w; int ldw, col0;
  if (colg < NQ) { w = wq; ldw = NQ; col0 = colg; }
  else if (colg < NQ + DF) { w = wg; ldw = DF; col0 = colg - NQ; }
  else { w = wu; ldw = DF; col0 = colg - NQ - DF; }
  u32x4 wv[9];
#pragma unroll
  for (int i = 0; i < 9; ++i) wv[i] = *reinterpret_cast<const u32x4*>(w + (size_t)(rg + 64 * i) * ldw + col0 + cc * 8);
  for (int i = tid; i < 2 * DM; i += 256) xs[i / DM][i % DM] = bf2f(x[i]);
  __syncthreads();
  float a0[8], a1[8];
#pragma unroll
  for (int k = 0; k < 8; ++k) { a0[k] = 0.f; a1[k] = 0.f; }
#pragma unroll
  for (int i = 0; i < 9; ++i) { const float x0 = xs[0][rg + 64 * i], x1 = xs[1][rg + 64 * i]; FMA8(a0, x0, wv[i]); FMA8(a1, x1, wv[i]); }
#pragma unroll
  for (int k = 0; k < 8; ++k) {
#pragma unroll
    for (int o = 4; o < 64; o <<= 1) { a0[k] += __shfl_xor(a0[k], o); a1[k] += __shfl_xor(a1[k], o); }
  }
  if (lane < 4) {
#pragma unroll
    for (int k = 0; k < 8; ++k) { red[wave][0][lane * 8 + k] = a0[k]; red[wave][1][lane * 8 + k] = a1[k]; }
  }
  __syncthreads();
  if (tid < 64) {
    const int b = tid >> 5, c = tid & 31;
    const float sv = (red[0][b][c] + red[1][b][c]) + (red[2][b][c] + red[3][b][c]);
    if (colg < NQ) reinterpret_cast<unsigned short*>(ws + WS_Q)[b * NQ + colg + c] = f2bf(sv);
    else reinterpret_cast<float*>(ws + WS_GU)[(size_t)b * 2 * DF + (colg - NQ) + c] = rbf(sv);
  }
}

__global__ void __launch_bounds__(NTHR, 4) k_attn(const unsigned short* __restrict__ kc, const unsigned short* __restrict__ vc,
                                                  unsigned char* __restrict__ ws) {
  __shared__ __attribute__((aligned(16))) float sm[SM_FLOATS];
  const int tid = threadIdx.x, lane = tid & 63, wave = __builtin_amdgcn_readfirstlane(tid >> 6);
  const int id = blockIdx.x;
  {
    const int pair = (id & 7) + 8 * ((id >> 3) & 3), split = id >> 5;
    const int b = pair >> 4, kvh = pair & 15;
    const int g = lane >> 4, c = lane & 15;
    constexpr float C2 = 0.088388347648318440f * 1.4426950408889634f;
    const size_t head_off = ((size_t)pair * SK + (size_t)split * KEYS_PER_WG) * DH;
    const unsigned short* kp = kc + head_off + (size_t)c * DH + 8 * g;
    const unsigned short* vp = vc + head_off + (size_t)(4 * g) * DH + 8 * c;
    u32x4 Kn[4], Vn[4];
#define ISSUE(t_) do { const size_t o_ = (size_t)(t_) * 16 * DH; \
    _Pragma("unroll") for (int j_ = 0; j_ < 4; ++j_) Kn[j_] = __builtin_nontemporal_load(reinterpret_cast<const u32x4*>(kp + o_ + 32 * j_)); \
    _Pragma("unroll") for (int j_ = 0; j_ < 4; ++j_) Vn[j_] = __builtin_nontemporal_load(reinterpret_cast<const u32x4*>(vp + o_ + (size_t)j_ * DH)); } while (0)
    ISSUE(wave);
    bf16x8 qb[4];
    {
      const unsigned short* q = reinterpret_cast<const unsigned short*>(ws + WS_Q) + (size_t)b * NQ + (size_t)(kvh * 2 + (c & 1)) * DH;
#pragma unroll
      for (int j = 0; j < 4; ++j) qb[j] = __builtin_bit_cast(bf16x8, *reinterpret_cast<const u32x4*>(q + 32 * j + 8 * g));
    }
    float m = -INFINITY, l = 0.f;
    f32x2 accA[4], accB[4];
#pragma unroll
    for (int k = 0; k < 4; ++k) { accA[k] = f32x2{0.f, 0.f}; accB[k] = f32x2{0.f, 0.f}; }
    for (int it = 0; it < TILES_PER_WAVE; ++it) {
      u32x4 Kc[4], Vc[4];
#pragma unroll
      for (int j = 0; j < 4; ++j) { Kc[j] = Kn[j]; Vc[j] = Vn[j]; }
      if (it + 1 < TILES_PER_WAVE) ISSUE(wave + AW * (it + 1));
      f32x4 s = {0.f, 0.f, 0.f, 0.f};
#pragma unroll
      for (int j = 0; j < 4; ++j) s = __builtin_amdgcn_mfma_f32_16x16x32_bf16(__builtin_bit_cast(bf16x8, Kc[j]), qb[j], s, 0, 0, 0);
      const float pm = fmaxf(fmaxf(s[0], s[1]), fmaxf(s[2], s[3])) * C2;
      if (__builtin_amdgcn_ballot_w64(pm > m)) {
        const float mn = fmaxf(m, pm);
        const float al = __builtin_amdgcn_exp2f(m - mn);
        const float alo = dpp<XOR1>(al);
        m = mn; l *= al;
#pragma unroll
        for (int k = 0; k < 4; ++k) { accA[k] *= al; accB[k] *= alo; }
      }
      float p[4], po[4];
#pragma unroll
      for (int i = 0; i < 4; ++i) { p[i] = __builtin_amdgcn_exp2f(fmaf(s[i], C2, -m)); po[i] = dpp<XOR1>(p[i]); }
      l += (p[0] + p[1]) + (p[2] + p[3]);
#pragma unroll
      for (int i = 0; i < 4; ++i) {
#pragma unroll
        for (int k = 0; k < 4; ++k) {
          const f32x2 v = {bf_lo(Vc[i][k]), bf_hi(Vc[i][k])};
          accA[k] = __builtin_elementwise_fma(f32x2{p[i], p[i]}, v, accA[k]);
          accB[k] = __builtin_elementwise_fma(f32x2{po[i], po[i]}, v, accB[k]);
        }
      }
    }
#undef ISSUE
    float (*s_acc)[2][DH] = reinterpret_cast<float (*)[2][DH]>(sm);
    float (*s_ml)[2][2] = reinterpret_cast<float (*)[2][2]>(sm + AW * 4 * 2 * DH);
    {
      const int st = wave * 4 + g, hA = c & 1, hB = hA ^ 1;
      *reinterpret_cast<f32x4*>(&s_acc[st][hA][8 * c]) = f32x4{accA[0][0], accA[0][1], accA[1][0], accA[1][1]};
      *reinterpret_cast<f32x4*>(&s_acc[st][hA][8 * c + 4]) = f32x4{accA[2][0], accA[2][1], accA[3][0], accA[3][1]};
      *reinterpret_cast<f32x4*>(&s_acc[st][hB][8 * c]) = f32x4{accB[0][0], accB[0][1], accB[1][0], accB[1][1]};
      *reinterpret_cast<f32x4*>(&s_acc[st][hB][8 * c + 4]) = f32x4{accB[2][0], accB[2][1], accB[3][0], accB[3][1]};
      if (c < 2) { s_ml[st][c][0] = m; s_ml[st][c][1] = l; }
    }
    __syncthreads();
    if (tid < 256) {
      const int h = tid >> 7, d = tid & 127;
      float M = -INFINITY;
#pragma unroll
      for (int s2 = 0; s2 < AW * 4; ++s2) M = fmaxf(M, s_ml[s2][h][0]);
      float L = 0.f, o = 0.f;
#pragma unroll
      for (int s2 = 0; s2 < AW * 4; ++s2) {
        const float w = __builtin_amdgcn_exp2f(s_ml[s2][h][0] - M);
        L = fmaf(w, s_ml[s2][h][1], L); o = fmaf(w, s_acc[s2][h][d], o);
      }
      const size_t slot = ((size_t)(pair * 2 + h)) * NSPLIT + split;
      reinterpret_cast<float*>(ws + WS_PO)[slot * DH + d] = o;
      if (d == 0) { float* ml = reinterpret_cast<float*>(ws + WS_ML) + slot * 2; ml[0] = M; ml[1] = L; }
    }
  }
}

__global__ void __launch_bounds__(576) k_post(const unsigned short* __restrict__ x, const unsigned short* __restrict__ wo,
                                              const unsigned short* __restrict__ wd, const unsigned short* __restrict__ gw,
                                              unsigned short* __restrict__ out, unsigned char* __restrict__ ws) {
  __shared__ float vin[2][128];
  __shared__ __attribute__((aligned(16))) float red[8][2][DM];
  __shared__ unsigned s_flag;
  const int tid = threadIdx.x, lane = tid & 63, wave = tid >> 6, blk = blockIdx.x;
  const bool is_a = blk < NPART_A;
  const unsigned short* w = is_a ? wo + (size_t)blk * 128 * DM : wd + (size_t)(blk - NPART_A) * 128 * DM;
  const int cc = tid % 72, rg = tid / 72;
  u32x4 wv[16];
  if (is_a) {
#pragma unroll
    for (int i = 0; i < 16; ++i) wv[i] = *reinterpret_cast<const u32x4*>(w + (size_t)(rg + 8 * i) * DM + cc * 8);
    if (tid < 256) {
      const int b = tid >> 7, d = tid & 127;
      const size_t row = (size_t)b * HQ + blk;
      const float* ml = reinterpret_cast<const float*>(ws + WS_ML) + row * NSPLIT * 2;
      const float* po = reinterpret_cast<const float*>(ws + WS_PO) + row * NSPLIT * DH + d;
      float mv[NSPLIT], lv[NSPLIT], pv[NSPLIT];
#pragma unroll
      for (int s = 0; s < NSPLIT; ++s) { mv[s] = ml[2 * s]; lv[s] = ml[2 * s + 1]; pv[s] = po[(size_t)s * DH]; }
      float M = -INFINITY;
#pragma unroll
      for (int s = 0; s < NSPLIT; ++s) M = fmaxf(M, mv[s]);
      float L = 0.f, o = 0.f;
#pragma unroll
      for (int s = 0; s < NSPLIT; ++s) { const float wgt = __builtin_amdgcn_exp2f(mv[s] - M); L = fmaf(wgt, lv[s], L); o = fmaf(wgt, pv[s], o); }
      vin[b][d] = rbf(o / L);
    }
  } else {
#pragma unroll
    for (int i = 0; i < 16; ++i) wv[i] = *reinterpret_cast<const u32x4*>(w + (size_t)(rg + 8 * i) * DM + cc * 8);
    if (tid < 256) {
      const int b = tid >> 7, f = (blk - NPART_A) * 128 + (tid & 127);
      const float* gu = reinterpret_cast<const float*>(ws + WS_GU) + (size_t)b * 2 * DF;
      const float gv = gu[f], uv = gu[DF + f];
      const float t = tanhf(0.7978845608028654f * (gv + 0.044715f * gv * gv * gv));
      vin[b][tid & 127] = rbf(rbf(0.5f * gv * (1.f + t)) * uv);
    }
  }
  __syncthreads();
  float a0[8], a1[8];
#pragma unroll
  for (int k = 0; k < 8; ++k) { a0[k] = 0.f; a1[k] = 0.f; }
#pragma unroll
  for (int i = 0; i < 16; ++i) { const float x0 = vin[0][rg + 8 * i], x1 = vin[1][rg + 8 * i]; FMA8(a0, x0, wv[i]); FMA8(a1, x1, wv[i]); }
#pragma unroll
  for (int k = 0; k < 8; ++k) { red[rg][0][cc * 8 + k] = a0[k]; red[rg][1][cc * 8 + k] = a1[k]; }
  __syncthreads();
  float* part = reinterpret_cast<float*>(ws + WS_PART) + (size_t)blk * 2 * DM;
  for (int i = tid; i < 2 * DM; i += 576) {
    const int b = i / DM, c = i % DM;
    float s = 0.f;
#pragma unroll
    for (int r = 0; r < 8; ++r) s += red[r][b][c];
    st_agent(part + i, s);
  }
  asm volatile("s_waitcnt vmcnt(0)" ::: "memory");
  __syncthreads();
  if (tid == 0) {
    const unsigned old = __hip_atomic_fetch_add(&g_ticket, 1u, __ATOMIC_RELAXED, __HIP_MEMORY_SCOPE_AGENT);
    const unsigned last = (old % NPOST == NPOST - 1) ? 1u : 0u;
    if (last) __builtin_amdgcn_fence(__ATOMIC_ACQUIRE, "agent");
    s_flag = last;
  }
  __syncthreads();
  if (s_flag == 0u) return;
  {
    const int qd = tid / 288, j = tid % 288;
    const f32x4* pp = reinterpret_cast<const f32x4*>(ws + WS_PART) + (size_t)(qd ? NPART_A : 0) * 288 + j;
    f32x4 t[NPART_B];
#pragma unroll
    for (int s = 0; s < NPART_B; ++s) t[s] = (qd || s < NPART_A) ? pp[(size_t)s * 288] : f32x4{0.f, 0.f, 0.f, 0.f};
    const float zt = rbf(bf2f(x[tid]) * bf2f(gw[tid])), zu = rbf(bf2f(x[DM + tid]) * bf2f(gw[tid]));
    f32x4 acc = t[0];
#pragma unroll
    for (int s = 1; s < NPART_B; ++s) acc += t[s];
    *reinterpret_cast<f32x4*>(&red[qd][0][0] + 4 * j) = acc;
    float z0 = zt, z1 = zu;
#pragma unroll
    for (int o = 1; o < 64; o <<= 1) { z0 += __shfl_xor(z0, o); z1 += __shfl_xor(z1, o); }
    if (lane == 0) { vin[0][wave] = z0; vin[1][wave] = z1; }
  }
  __syncthreads();
#pragma unroll
  for (int b = 0; b < 2; ++b) {
    float z = 0.f;
#pragma unroll
    for (int w2 = 0; w2 < 9; ++w2) z += vin[b][w2];
    z = rbf(z);
    const float gt = rbf(1.f / (1.f + __expf(-z)));
    const float av = rbf(red[0][b][tid]), bv = rbf(red[1][b][tid]);
    out[b * DM + tid] = f2bf(rbf(rbf(gt * av) + rbf(rbf(1.f - gt) * bv)));
  }
}

}

extern "C" void kernel_launch(void* const* d_in, const int* in_sizes, int n_in, void* d_out, int out_size, void* d_ws,
                              size_t ws_size, hipStream_t stream) {
  const unsigned short* x = (const unsigned short*)d_in[0];
  unsigned char* ws = (unsigned char*)d_ws;
  k_q<<<(NQ + 2 * DF) / 32, 256, 0, stream>>>(x, (const unsigned short*)d_in[3], (const unsigned short*)d_in[6], (const unsigned short*)d_in[7], ws);
  k_attn<<<NATT, NTHR, 0, stream>>>((const unsigned short*)d_in[1], (const unsigned short*)d_in[2], ws);
  k_post<<<NPOST, 576, 0, stream>>>(x, (const unsigned short*)d_in[4], (const unsigned short*)d_in[8], (const unsigned short*)d_in[9],
                                    (unsigned short*)d_out, ws);
}
```

```cpp
#include <hip/hip_runtime.h>
#include <hip/hip_bf16.h>
#include <cstdint>

namespace {

constexpr int BATCH = 2, DM = 576, HQ = 32, HKV = 16, SK = 32768, DH = 128, DF = 4608;
constexpr int NQ = HQ * DH;
constexpr int NCOL1 = NQ + 2 * DF;

constexpr int NSPLIT = 8;
constexpr int AW = 8;
constexpr int ATHR = AW * 64;
constexpr int KEYS_PER_WG = SK / NSPLIT;
constexpr int TILES_PER_WAVE = KEYS_PER_WG / 16 / AW;

constexpr size_t WS_Q = 0;
constexpr size_t WS_GU = WS_Q + (size_t)BATCH * NQ * 2;
constexpr size_t WS_ML = WS_GU + (size_t)BATCH * 2 * DF * 4;
constexpr size_t WS_PO = WS_ML + (size_t)BATCH * HQ * NSPLIT * 2 * 4;
constexpr size_t WS_PART = WS_PO + (size_t)BATCH * HQ * NSPLIT * DH * 4;
constexpr int NPART_A = HQ, NPART_B = DF / 128;

using bf16x8 = __attribute__((ext_vector_type(8))) short;
using f32x4 = __attribute__((ext_vector_type(4))) float;
using f32x2 = __attribute__((ext_vector_type(2))) float;
using u32x4 = __attribute__((ext_vector_type(4))) unsigned;

__device__ __forceinline__ float bf_lo(unsigned u) { return __uint_as_float(u << 16); }
__device__ __forceinline__ float bf_hi(unsigned u) { return __uint_as_float(u & 0xffff0000u); }
__device__ __forceinline__ float bf2f(unsigned short h) { return __uint_as_float((unsigned)h << 16); }
__device__ __forceinline__ unsigned short f2bf(float f) {
  unsigned u = __float_as_uint(f);
  u += 0x7fffu + ((u >> 16) & 1u);
  return (unsigned short)(u >> 16);
}
__device__ __forceinline__ float rbf(float f) { return bf2f(f2bf(f)); }
template <int CTRL> __device__ __forceinline__ float dpp(float x) {
  return __builtin_bit_cast(float, __builtin_amdgcn_mov_dpp(__builtin_bit_cast(int, x), CTRL, 0xf, 0xf, true));
}
constexpr int XOR1 = 0xB1;

__global__ void __launch_bounds__(256) k_pre(const unsigned short* __restrict__ x, const unsigned short* __restrict__ wq,
                                             const unsigned short* __restrict__ wg, const unsigned short* __restrict__ wu,
                                             unsigned char* __restrict__ ws) {
  __shared__ float xs[2][DM];
  __shared__ float red[4][2][64];
  const int tid = threadIdx.x, lane = tid & 63, wave = tid >> 6;
  for (int i = tid; i < 2 * DM; i += 256) xs[i / DM][i % DM] = bf2f(x[i]);
  const int col0 = blockIdx.x * 64;
  const unsigned short* w; int ldw, c0;
  if (col0 < NQ) { w = wq; ldw = NQ; c0 = col0; }
  else if (col0 < NQ + DF) { w = wg; ldw = DF; c0 = col0 - NQ; }
  else { w = wu; ldw = DF; c0 = col0 - NQ - DF; }
  const int cc = tid & 7, rg = tid >> 3;
  u32x4 wv[18];
#pragma unroll
  for (int i = 0; i < 18; ++i) wv[i] = *reinterpret_cast<const u32x4*>(w + (size_t)(rg + 32 * i) * ldw + c0 + cc * 8);
  __syncthreads();
  float a0[8], a1[8];
#pragma unroll
  for (int k = 0; k < 8; ++k) { a0[k] = 0.f; a1[k] = 0.f; }
#pragma unroll
  for (int i = 0; i < 18; ++i) {
    const float x0 = xs[0][rg + 32 * i], x1 = xs[1][rg + 32 * i];
#pragma unroll
    for (int k = 0; k < 4; ++k) {
      const float lo = bf_lo(wv[i][k]), hi = bf_hi(wv[i][k]);
      a0[2 * k] = fmaf(x0, lo, a0[2 * k]); a0[2 * k + 1] = fmaf(x0, hi, a0[2 * k + 1]);
      a1[2 * k] = fmaf(x1, lo, a1[2 * k]); a1[2 * k + 1] = fmaf(x1, hi, a1[2 * k + 1]);
    }
  }
#pragma unroll
  for (int k = 0; k < 8; ++k) {
#pragma unroll
    for (int o = 8; o < 64; o <<= 1) { a0[k] += __shfl_xor(a0[k], o); a1[k] += __shfl_xor(a1[k], o); }
  }
  if (lane < 8) {
#pragma unroll
    for (int k = 0; k < 8; ++k) { red[wave][0][lane * 8 + k] = a0[k]; red[wave][1][lane * 8 + k] = a1[k]; }
  }
  __syncthreads();
  if (tid < 128) {
    const int b = tid >> 6, c = tid & 63;
    const float s = (red[0][b][c] + red[1][b][c]) + (red[2][b][c] + red[3][b][c]);
    if (col0 < NQ) reinterpret_cast<unsigned short*>(ws + WS_Q)[b * NQ + col0 + c] = f2bf(s);
    else reinterpret_cast<float*>(ws + WS_GU)[b * 2 * DF + (col0 - NQ) + c] = rbf(s);
  }
}

__global__ void __launch_bounds__(ATHR) k_attn(const unsigned short* __restrict__ kc, const unsigned short* __restrict__ vc,
                                               unsigned char* __restrict__ ws) {
  __shared__ __attribute__((aligned(16))) float s_acc[AW * 4][2][DH];
  __shared__ float s_ml[AW * 4][2][2];
  const int split = blockIdx.x, kvh = blockIdx.y, b = blockIdx.z;
  const int tid = threadIdx.x, lane = tid & 63, wave = __builtin_amdgcn_readfirstlane(tid >> 6);
  const int g = lane >> 4, c = lane & 15;
  constexpr float C2 = 0.088388347648318440f * 1.4426950408889634f;

  bf16x8 qb[4];
  {
    const unsigned short* q = reinterpret_cast<const unsigned short*>(ws + WS_Q) + (size_t)b * NQ + (size_t)(kvh * 2 + (c & 1)) * DH;
#pragma unroll
    for (int j = 0; j < 4; ++j) qb[j] = __builtin_bit_cast(bf16x8, *reinterpret_cast<const u32x4*>(q + 32 * j + 8 * g));
  }
  const size_t head_off = ((size_t)(b * HKV + kvh) * SK + (size_t)split * KEYS_PER_WG) * DH;
  const unsigned short* kp = kc + head_off + (size_t)c * DH + 8 * g;
  const unsigned short* vp = vc + head_off + (size_t)(4 * g) * DH + 8 * c;

  u32x4 Kn[4], Vn[4];
#define ISSUE(t_) do { const size_t o_ = (size_t)(t_) * 16 * DH; \
    _Pragma("unroll") for (int j_ = 0; j_ < 4; ++j_) Kn[j_] = __builtin_nontemporal_load(reinterpret_cast<const u32x4*>(kp + o_ + 32 * j_)); \
    _Pragma("unroll") for (int j_ = 0; j_ < 4; ++j_) Vn[j_] = __builtin_nontemporal_load(reinterpret_cast<const u32x4*>(vp + o_ + (size_t)j_ * DH)); } while (0)

  float m = -INFINITY, l = 0.f;
  f32x2 accA[4], accB[4];
#pragma unroll
  for (int k = 0; k < 4; ++k) { accA[k] = f32x2{0.f, 0.f}; accB[k] = f32x2{0.f, 0.f}; }

  ISSUE(wave);
  for (int it = 0; it < TILES_PER_WAVE; ++it) {
    u32x4 Kc[4], Vc[4];
#pragma unroll
    for (int j = 0; j < 4; ++j) { Kc[j] = Kn[j]; Vc[j] = Vn[j]; }
    if (it + 1 < TILES_PER_WAVE) ISSUE(wave + AW * (it + 1));
    f32x4 s = {0.f, 0.f, 0.f, 0.f};
#pragma unroll
    for (int j = 0; j < 4; ++j) s = __builtin_amdgcn_mfma_f32_16x16x32_bf16(__builtin_bit_cast(bf16x8, Kc[j]), qb[j], s, 0, 0, 0);
    const float pm = fmaxf(fmaxf(s[0], s[1]), fmaxf(s[2], s[3])) * C2;
    if (__builtin_amdgcn_ballot_w64(pm > m)) {
      const float mn = fmaxf(m, pm);
      const float al = __builtin_amdgcn_exp2f(m - mn);
      const float alo = dpp<XOR1>(al);
      m = mn; l *= al;
#pragma unroll
      for (int k = 0; k < 4; ++k) { accA[k] *= al; accB[k] *= alo; }
    }
    float p[4], po[4];
#pragma unroll
    for (int i = 0; i < 4; ++i) { p[i] = __builtin_amdgcn_exp2f(fmaf(s[i], C2, -m)); po[i] = dpp<XOR1>(p[i]); }
    l += (p[0] + p[1]) + (p[2] + p[3]);
#pragma unroll
    for (int i = 0; i < 4; ++i) {
#pragma unroll
      for (int k = 0; k < 4; ++k) {
        const f32x2 v = {bf_lo(Vc[i][k]), bf_hi(Vc[i][k])};
        accA[k] = __builtin_elementwise_fma(f32x2{p[i], p[i]}, v, accA[k]);
        accB[k] = __builtin_elementwise_fma(f32x2{po[i], po[i]}, v, accB[k]);
      }
    }
  }
#undef ISSUE
  const int st = wave * 4 + g;
  {
    const int hA = c & 1, hB = hA ^ 1;
    *reinterpret_cast<f32x4*>(&s_acc[st][hA][8 * c]) = f32x4{accA[0][0], accA[0][1], accA[1][0], accA[1][1]};
    *reinterpret_cast<f32x4*>(&s_acc[st][hA][8 * c + 4]) = f32x4{accA[2][0], accA[2][1], accA[3][0], accA[3][1]};
    *reinterpret_cast<f32x4*>(&s_acc[st][hB][8 * c]) = f32x4{accB[0][0], accB[0][1], accB[1][0], accB[1][1]};
    *reinterpret_cast<f32x4*>(&s_acc[st][hB][8 * c + 4]) = f32x4{accB[2][0], accB[2][1], accB[3][0], accB[3][1]};
    if (c < 2) { s_ml[st][c][0] = m; s_ml[st][c][1] = l; }
  }
  __syncthreads();
  if (tid < 256) {
    const int h = tid >> 7, d = tid & 127;
    float M = -INFINITY;
#pragma unroll
    for (int s2 = 0; s2 < AW * 4; ++s2) M = fmaxf(M, s_ml[s2][h][0]);
    float L = 0.f, o = 0.f;
#pragma unroll
    for (int s2 = 0; s2 < AW * 4; ++s2) {
      const float w = __builtin_amdgcn_exp2f(s_ml[s2][h][0] - M);
      L = fmaf(w, s_ml[s2][h][1], L); o = fmaf(w, s_acc[s2][h][d], o);
    }
    const size_t slot = ((size_t)(b * HQ + kvh * 2 + h)) * NSPLIT + split;
    reinterpret_cast<float*>(ws + WS_PO)[slot * DH + d] = o;
    if (d == 0) { float* ml = reinterpret_cast<float*>(ws + WS_ML) + slot * 2; ml[0] = M; ml[1] = L; }
  }
}

__global__ void __launch_bounds__(576) k_post(const unsigned short* __restrict__ wo, const unsigned short* __restrict__ wd,
                                              unsigned char* __restrict__ ws) {
  __shared__ float vin[2][128];
  __shared__ float red[8][2][DM];
  const int tid = threadIdx.x, blk = blockIdx.x;
  const bool is_a = blk < NPART_A;
  const unsigned short* w = is_a ? wo + (size_t)blk * 128 * DM : wd + (size_t)(blk - NPART_A) * 128 * DM;
  const int cc = tid % 72, rg = tid / 72;
  u32x4 wv[16];
#pragma unroll
  for (int i = 0; i < 16; ++i) wv[i] = *reinterpret_cast<const u32x4*>(w + (size_t)(rg + 8 * i) * DM + cc * 8);
  if (tid < 256) {
    const int b = tid >> 7, d = tid & 127;
    float val;
    if (is_a) {
      const size_t row = (size_t)b * HQ + blk;
      const float* ml = reinterpret_cast<const float*>(ws + WS_ML) + row * NSPLIT * 2;
      const float* po = reinterpret_cast<const float*>(ws + WS_PO) + row * NSPLIT * DH + d;
      float M = -INFINITY;
#pragma unroll
      for (int s = 0; s < NSPLIT; ++s) M = fmaxf(M, ml[2 * s]);
      float L = 0.f, o = 0.f;
#pragma unroll
      for (int s = 0; s < NSPLIT; ++s) { const float wgt = __builtin_amdgcn_exp2f(ml[2 * s] - M); L = fmaf(wgt, ml[2 * s + 1], L); o = fmaf(wgt, po[(size_t)s * DH], o); }
      val = rbf(o / L);
    } else {
      const int f = (blk - NPART_A) * 128 + d;
      const float* gu = reinterpret_cast<const float*>(ws + WS_GU) + (size_t)b * 2 * DF;
      const float gv = gu[f], uv = gu[DF + f];
      const float t = tanhf(0.7978845608028654f * (gv + 0.044715f * gv * gv * gv));
      val = rbf(rbf(0.5f * gv * (1.f + t)) * uv);
    }
    vin[b][d] = val;
  }
  __syncthreads();
  float a0[8], a1[8];
#pragma unroll
  for (int k = 0; k < 8; ++k) { a0[k] = 0.f; a1[k] = 0.f; }
#pragma unroll
  for (int i = 0; i < 16; ++i) {
    const float x0 = vin[0][rg + 8 * i], x1 = vin[1][rg + 8 * i];
#pragma unroll
    for (int k = 0; k < 4; ++k) {
      const float lo = bf_lo(wv[i][k]), hi = bf_hi(wv[i][k]);
      a0[2 * k] = fmaf(x0, lo, a0[2 * k]); a0[2 * k + 1] = fmaf(x0, hi, a0[2 * k + 1]);
      a1[2 * k] = fmaf(x1, lo, a1[2 * k]); a1[2 * k + 1] = fmaf(x1, hi, a1[2 * k + 1]);
    }
  }
#pragma unroll
  for (int k = 0; k < 8; ++k) { red[rg][0][cc * 8 + k] = a0[k]; red[rg][1][cc * 8 + k] = a1[k]; }
  __syncthreads();
  float* part = reinterpret_cast<float*>(ws + WS_PART) + (size_t)blk * 2 * DM;
  for (int i = tid; i < 2 * DM; i += 576) {
    const int b = i / DM, c = i % DM;
    float s = 0.f;
#pragma unroll
    for (int r = 0; r < 8; ++r) s += red[r][b][c];
    part[i] = s;
  }
}

__global__ void __launch_bounds__(576) k_final(const unsigned short* __restrict__ x, const unsigned short* __restrict__ gw,
                                               const unsigned char* __restrict__ ws, unsigned short* __restrict__ out) {
  __shared__ float red[9];
  const int b = blockIdx.x, c = threadIdx.x;
  float t = rbf(bf2f(x[b * DM + c]) * bf2f(gw[c]));
#pragma unroll
  for (int o = 1; o < 64; o <<= 1) t += __shfl_xor(t, o);
  if ((c & 63) == 0) red[c >> 6] = t;
  const float* part = reinterpret_cast<const float*>(ws + WS_PART) + (size_t)b * DM + c;
  float a = 0.f, bb = 0.f;
#pragma unroll
  for (int s = 0; s < NPART_A; ++s) a += part[(size_t)s * 2 * DM];
#pragma unroll
  for (int s = 0; s < NPART_B; ++s) bb += part[(size_t)(NPART_A + s) * 2 * DM];
  __syncthreads();
  float z = 0.f;
#pragma unroll
  for (int i = 0; i < 9; ++i) z += red[i];
  z = rbf(z);
  const float gt = rbf(1.f / (1.f + __expf(-z)));
  a = rbf(a); bb = rbf(bb);
  const float r = rbf(rbf(gt * a) + rbf(rbf(1.f - gt) * bb));
  out[b * DM + c] = f2bf(r);
}

}

extern "C" void kernel_launch(void* const* d_in, const int* in_sizes, int n_in, void* d_out, int out_size, void* d_ws,
                              size_t ws_size, hipStream_t stream) {
  const unsigned short* x = (const unsigned short*)d_in[0];
  const unsigned short* kc = (const unsigned short*)d_in[1];
  const unsigned short* vc = (const unsigned short*)d_in[2];
  const unsigned short* wq = (const unsigned short*)d_in[3];
  const unsigned short* wo = (const unsigned short*)d_in[4];
  const unsigned short* wg = (const unsigned short*)d_in[6];
  const unsigned short* wu = (const unsigned short*)d_in[7];
  const unsigned short* wd = (const unsigned short*)d_in[8];
  const unsigned short* gw = (const unsigned short*)d_in[9];
  unsigned char* ws = (unsigned char*)d_ws;
  k_pre<<<NCOL1 / 64, 256, 0, stream>>>(x, wq, wg, wu, ws);
  k_attn<<<dim3(NSPLIT, HKV, BATCH), ATHR, 0, stream>>>(kc, vc, ws);
  k_post<<<NPART_A + NPART_B, 576, 0, stream>>>(wo, wd, ws);
  k_final<<<BATCH, 576, 0, stream>>>(x, gw, ws, (unsigned short*)d_out);
}
```
